```python
import torch

def _softmax(x, axis=-1):
    return torch.softmax(x, dim=axis)

def _gelu(x):
    return torch.nn.functional.gelu(x, approximate='tanh')

def _silu(x):
    return torch.nn.functional.silu(x)

def _sigmoid(x):
    return torch.sigmoid(x)

def _layernorm(x, axis=-1, eps=1e-05):
    m = x.mean(dim=axis, keepdim=True)
    v = x.var(dim=axis, keepdim=True, unbiased=False)
    return (x - m) / torch.sqrt(v + eps)

def _scatter_add(tgt, idx, src, axis=0):
    return tgt.clone().index_add_(axis, idx, src)

def _segment_sum(src, seg, n):
    out = torch.zeros((n,) + src.shape[1:], dtype=src.dtype)
    return out.index_add_(0, seg, src)

def _pad(x, pw):
    flat = tuple((v for pair in reversed(pw) for v in pair))
    return torch.nn.functional.pad(x, flat)
kernel_name = 'adaLN_cond__sliding_block_cached__geglu_ffn__decode'

def setup_inputs():

    _w = lambda a: (lambda t: t.to(torch.bfloat16) if t.is_floating_point() else t)(torch.as_tensor(a))

    def _A_setup():
        B, Sq, D = (4, 1, 512)
        Hq, Hkv, Sk, Dh = (32, 8, 16384, 128)
        assert Hq % Hkv == 0, (Hq, Hkv)
        return {'x': _w((lambda *s: torch.randn(*s, dtype=torch.bfloat16) * 0.05)(B, Sq, D)), 'k_cache': _w((lambda *s: torch.randn(*s, dtype=torch.bfloat16) * 1.6)(B, Hkv, Sk, Dh)), 'v_cache': _w((lambda *s: torch.randn(*s, dtype=torch.bfloat16) * 1.6)(B, Hkv, Sk, Dh)), 'wq': _w((lambda *s: torch.randn(*s, dtype=torch.bfloat16) * 1.6)(D, Hq * Dh) * D ** (-0.5)), 'wo': _w((lambda *s: torch.randn(*s, dtype=torch.bfloat16) * 1.6)(Hq * Dh, D) * (Hq * Dh) ** (-0.5))}
    _A_in = _A_setup()

    def _B_setup():
        B, S, D, F = (4, 1, 512, 1792)
        return {'x': _w((lambda *s: torch.randn(*s, dtype=torch.bfloat16) * 0.05)(B, S, D)), 'w_gate': _w((lambda *s: torch.randn(*s, dtype=torch.bfloat16) * 0.05)(D, F)), 'w_up': _w((lambda *s: torch.randn(*s, dtype=torch.bfloat16) * 0.05)(D, F)), 'w_down': _w((lambda *s: torch.randn(*s, dtype=torch.bfloat16) * 0.05)(F, D))}
    _B_in = _B_setup()
    return {'A_x': _A_in['x'], 'A_k_cache': _A_in['k_cache'], 'A_v_cache': _A_in['v_cache'], 'A_wq': _A_in['wq'], 'A_wo': _A_in['wo'], 'B_x': _B_in['x'], 'B_w_gate': _B_in['w_gate'], 'B_w_up': _B_in['w_up'], 'B_w_down': _B_in['w_down'], 'c': _w((lambda *s: torch.randn(*s, dtype=torch.bfloat16) * 0.2)(4, 512) * 8.0), 'w_mod': _w((lambda *s: torch.randn(*s, dtype=torch.bfloat16) * 0.2)(512, 6 * 512) * 512 ** (-0.5) * 8.0)}

def reference(A_x, A_k_cache, A_v_cache, A_wq, A_wo, B_x, B_w_gate, B_w_up, B_w_down, c, w_mod):

    def _A_ref(x, k_cache, v_cache, wq, wo):
        B, Sq, D = x.shape
        Hq, Dh = (32, 128)
        Hkv, Sk = (k_cache.shape[1], k_cache.shape[2])
        W = min(4096, max(256, Sk // 4))
        rep = Hq // Hkv
        q = (lambda x, *d: x.permute(*d))((x @ wq).reshape(B, Sq, Hq, Dh), 0, 2, 1, 3)
        k = (lambda x, n, axis: torch.repeat_interleave(x, n, dim=axis))(k_cache[:, :, Sk - W:, :], rep, axis=1)
        v = (lambda x, n, axis: torch.repeat_interleave(x, n, dim=axis))(v_cache[:, :, Sk - W:, :], rep, axis=1)
        scores = torch.einsum('bhqd,bhkd->bhqk', q, k) * Dh ** (-0.5)
        p = _softmax(scores, axis=-1)
        o = torch.einsum('bhqk,bhkd->bhqd', p, v)
        return (lambda x, *d: x.permute(*d))(o, 0, 2, 1, 3).reshape(B, Sq, Hq * Dh) @ wo

    def _B_ref(x, w_gate, w_up, w_down):
        return _gelu(x @ w_gate) * (x @ w_up) @ w_down
    x = A_x
    mod = (_silu(c) @ w_mod).reshape(c.shape[0], 6, 1, -1)
    sa, ca, ga, sb, cb, gb = (mod[:, 0].clone(), mod[:, 1].clone(), mod[:, 2].clone(), mod[:, 3].clone(), mod[:, 4].clone(), mod[:, 5].clone())
    h = _layernorm(x, axis=-1) * (1 + ca) + sa
    x = x + ga * _A_ref(h, A_k_cache, A_v_cache, A_wq, A_wo)
    h = _layernorm(x, axis=-1) * (1 + cb) + sb
    x = x + gb * _B_ref(h, B_w_gate, B_w_up, B_w_down)
    return x
```

```cpp
#include <hip/hip_runtime.h>
#include <hip/hip_bf16.h>
#include <stdint.h>

typedef unsigned short u16;
__device__ __forceinline__ float bf2f(u16 b) { return __uint_as_float(((unsigned)b) << 16); }
__device__ __forceinline__ u16 f2bf(float f) { __hip_bfloat16 h = __float2bfloat16(f); return *(u16*)&h; }
__device__ __forceinline__ float rb(float f) { return bf2f(f2bf(f)); }

constexpr int B = 4, D = 512, HQ = 32, HKV = 8, SK = 16384, DH = 128, W = 4096, F = 1792;

template <int K>
__device__ void gemv_cols(const float* a, const u16* w, int N, int n, float acc[4]) {
  for (int b = 0; b < 4; ++b) acc[b] = 0.f;
  for (int k = 0; k < K; ++k) {
    float wv = bf2f(w[(size_t)k * N + n]);
    for (int b = 0; b < 4; ++b) acc[b] += a[b * K + k] * wv;
  }
}

__global__ void k_mod(const u16* c, const u16* wmod, float* mod) {
  __shared__ float a[4 * 512];
  for (int i = threadIdx.x; i < 2048; i += blockDim.x) { float v = bf2f(c[i]); a[i] = rb(v / (1.f + __expf(-v))); }
  __syncthreads();
  int n = blockIdx.x * blockDim.x + threadIdx.x;
  float acc[4]; gemv_cols<512>(a, wmod, 3072, n, acc);
  for (int b = 0; b < 4; ++b) mod[b * 3072 + n] = rb(acc[b]);
}

__device__ void ln_mod(const float* x  , const float* mod, int soff, int coff, float* h  ) {
  int w = threadIdx.x >> 6, l = threadIdx.x & 63;
  if (w < 4) {
    float s = 0.f;
    for (int i = l; i < 512; i += 64) s += x[w * 512 + i];
    for (int m = 32; m >= 1; m >>= 1) s += __shfl_xor(s, m);
    float mean = s / 512.f;
    float v = 0.f;
    for (int i = l; i < 512; i += 64) { float d = x[w * 512 + i] - mean; v += d * d; }
    for (int m = 32; m >= 1; m >>= 1) v += __shfl_xor(v, m);
    float var = rb(v / 512.f);
    float mb = rb(mean);
    float den = rb(sqrtf(rb(var + 1e-5f)));
    for (int i = l; i < 512; i += 64) {
      float xn = rb(rb(x[w * 512 + i] - mb) / den);
      float t = rb(xn * rb(1.f + mod[w * 3072 + coff + i]));
      h[w * 512 + i] = rb(t + mod[w * 3072 + soff + i]);
    }
  }
}

__global__ void k_q(const u16* x, const float* mod, const u16* wq, float* q) {
  __shared__ float xs[2048], h[2048];
  for (int i = threadIdx.x; i < 2048; i += blockDim.x) xs[i] = bf2f(x[i]);
  __syncthreads();
  ln_mod(xs, mod, 0, 512, h);
  __syncthreads();
  int n = blockIdx.x * blockDim.x + threadIdx.x;
  float acc[4]; gemv_cols<512>(h, wq, 4096, n, acc);
  for (int b = 0; b < 4; ++b) q[b * 4096 + n] = rb(acc[b]);
}

__global__ void k_attn(const float* q, const u16* kc, const u16* vc, float* o) {
  __shared__ float qs[128], s[W], red[256];
  int b = blockIdx.x / HQ, h = blockIdx.x % HQ, kvh = h / 4, t = threadIdx.x;
  if (t < 128) qs[t] = q[b * 4096 + h * 128 + t];
  __syncthreads();
  const u16* kb = kc + ((size_t)(b * HKV + kvh) * SK + (SK - W)) * DH;
  const u16* vb = vc + ((size_t)(b * HKV + kvh) * SK + (SK - W)) * DH;
  float mx = -INFINITY;
  for (int j = t; j < W; j += 256) {
    float a = 0.f;
    for (int d = 0; d < 128; ++d) a += qs[d] * bf2f(kb[(size_t)j * DH + d]);
    a = rb(rb(a) * 0.08838834764831845f);
    s[j] = a; mx = fmaxf(mx, a);
  }
  red[t] = mx; __syncthreads();
  for (int m = 128; m >= 1; m >>= 1) { if (t < m) red[t] = fmaxf(red[t], red[t + m]); __syncthreads(); }
  mx = red[0]; __syncthreads();
  float sum = 0.f;
  for (int j = t; j < W; j += 256) { float e = __expf(s[j] - mx); s[j] = e; sum += e; }
  red[t] = sum; __syncthreads();
  for (int m = 128; m >= 1; m >>= 1) { if (t < m) red[t] += red[t + m]; __syncthreads(); }
  sum = red[0]; __syncthreads();
  for (int j = t; j < W; j += 256) s[j] = rb(s[j] / sum);
  __syncthreads();
  int d = t & 127, half = t >> 7;
  float a = 0.f;
  for (int j = half; j < W; j += 2) a += s[j] * bf2f(vb[(size_t)j * DH + d]);
  red[t] = a; __syncthreads();
  if (t < 128) o[b * 4096 + h * 128 + t] = rb(red[t] + red[t + 128]);
}

__global__ void k_wo(const float* o, const u16* wo, const u16* x, const float* mod, float* x2) {
  __shared__ float a[4 * 4096];
  for (int i = threadIdx.x; i < 4 * 4096; i += blockDim.x) a[i] = o[i];
  __syncthreads();
  int n = blockIdx.x * blockDim.x + threadIdx.x;
  float acc[4]; gemv_cols<4096>(a, wo, 512, n, acc);
  for (int b = 0; b < 4; ++b) x2[b * 512 + n] = rb(bf2f(x[b * 512 + n]) + rb(mod[b * 3072 + 1024 + n] * rb(acc[b])));
}

__global__ void k_gu(const float* x2, const float* mod, const u16* wg, const u16* wu, float* g) {
  __shared__ float xs[2048], h[2048];
  for (int i = threadIdx.x; i < 2048; i += blockDim.x) xs[i] = x2[i];
  __syncthreads();
  ln_mod(xs, mod, 3 * 512, 4 * 512, h);
  __syncthreads();
  int n = blockIdx.x * blockDim.x + threadIdx.x;
  float ag[4], au[4]; gemv_cols<512>(h, wg, F, n, ag); gemv_cols<512>(h, wu, F, n, au);
  for (int b = 0; b < 4; ++b) {
    float v = rb(ag[b]);
    float ge = rb(0.5f * v * (1.f + tanhf(0.7978845608028654f * (v + 0.044715f * v * v * v))));
    g[b * F + n] = rb(ge * rb(au[b]));
  }
}

__global__ void k_down(const float* g, const u16* wd, const float* x2, const float* mod, u16* out) {
  __shared__ float a[4 * F];
  for (int i = threadIdx.x; i < 4 * F; i += blockDim.x) a[i] = g[i];
  __syncthreads();
  int n = blockIdx.x * blockDim.x + threadIdx.x;
  float acc[4]; gemv_cols<F>(a, wd, 512, n, acc);
  for (int b = 0; b < 4; ++b) out[b * 512 + n] = f2bf(x2[b * 512 + n] + rb(mod[b * 3072 + 5 * 512 + n] * rb(acc[b])));
}

extern "C" void kernel_launch(void* const* d_in, const int* in_sizes, int n_in, void* d_out, int out_size, void* d_ws, size_t ws_size, hipStream_t stream) {
  const u16* x = (const u16*)d_in[0]; const u16* kc = (const u16*)d_in[1]; const u16* vc = (const u16*)d_in[2];
  const u16* wq = (const u16*)d_in[3]; const u16* wo = (const u16*)d_in[4];
  const u16* wg = (const u16*)d_in[6]; const u16* wu = (const u16*)d_in[7]; const u16* wd = (const u16*)d_in[8];
  const u16* c = (const u16*)d_in[9]; const u16* wmod = (const u16*)d_in[10];
  float* ws = (float*)d_ws;
  float* mod = ws; float* q = mod + 4 * 3072; float* o = q + 4 * 4096; float* x2 = o + 4 * 4096; float* g = x2 + 2048;
  k_mod<<<3072 / 64, 64, 0, stream>>>(c, wmod, mod);
  k_q<<<4096 / 256, 256, 0, stream>>>(x, mod, wq, q);
  k_attn<<<B * HQ, 256, 0, stream>>>(q, kc, vc, o);
  k_wo<<<512 / 64, 64, 0, stream>>>(o, wo, x, mod, x2);
  k_gu<<<F / 256, 256, 0, stream>>>(x2, mod, wg, wu, g);
  k_down<<<512 / 64, 64, 0, stream>>>(g, wd, x2, mod, (u16*)d_out);
}
```

```cpp
#include <hip/hip_runtime.h>
#include <stdint.h>

typedef unsigned short u16;
typedef unsigned v4u __attribute__((ext_vector_type(4)));
typedef unsigned v2u __attribute__((ext_vector_type(2)));
typedef float f32x4 __attribute__((ext_vector_type(4)));
typedef float f32x2 __attribute__((ext_vector_type(2)));
typedef short bf16x8 __attribute__((ext_vector_type(8)));
typedef short v4s __attribute__((ext_vector_type(4)));
typedef __bf16 bf16x2_t __attribute__((ext_vector_type(2)));
#define LAS __attribute__((address_space(3)))
typedef __amdgpu_buffer_rsrc_t rsrc_t;

constexpr int TOKWIN = 4;
constexpr int BUMP = 1;
constexpr int KLEAD = 2;
constexpr int QHOLD = 4;
constexpr int QPOLLAT = 18, QTIME = 460, QTROLE = 150;
constexpr int WPFAT = 3, WSL = 12;
constexpr int GDELAY = 2, DDELAY = 32, DPDELAY = 30, GGDELAY = 30, PDELAY = 170;
constexpr int CODE_PF = 21504;

constexpr int SK = 16384, WIN_KEYS = 4096, FF = 1792;
constexpr int NWG = 256, NTHR = 1024;
constexpr unsigned SPIN_CAP = 1u << 19;
constexpr int AUX_W = 0, AUX_KV = 2  , AUX_SC1 = 16;
constexpr unsigned SENT = 0xFFFFFFFFu;
constexpr int OOB = 0x40000000;

constexpr int WS_EPOCH = 0;
constexpr int WS_MOD = 4096, MOD_B = 768 * 16;
constexpr int WS_Q = WS_MOD + 4 * MOD_B, Q_B = 1024 * 16;
constexpr int WS_O = WS_Q + 4 * Q_B, O_REC = 1152;
constexpr int WS_P = WS_O + 256 * O_REC;
constexpr int WS_G = WS_P + 128 * 512;
constexpr int WS_F = WS_G + 56 * 512;
constexpr int WS_END = WS_F + 8 * 256;
static_assert(WS_END - WS_P <= 16 * 6 * 1024, "reset coverage");

constexpr int L_DONE = 0, L_QFLAG = 12, L_ECNT = 16, L_TGO = 20, L_HOLD = 24, L_PFGO = 28, L_MLC = 32, L_LSE = 36, L_TLC = 40, L_HUP = 44, L_CST = 4, L_MSEQ = 48  ;
constexpr int L_ZERO = 64  ;
constexpr int L_Q = 1024, L_ML = L_Q + 1024, L_LT = L_ML + 512, L_MRG = L_LT + 512, MRG_B = 2304, L_STAT = L_MRG + 4 * MRG_B, L_EPART = L_STAT + 2048, PSTR = 1088, L_WIMG = L_EPART + 8 * PSTR, L_SCR = L_WIMG + 4096;
constexpr int SCR_B = 4608, L_PART = L_SCR + 16 * SCR_B, L_PARTC = L_PART + 8 * PSTR, L_END = L_PARTC + 8 * PSTR;

struct Params {
  const u16 *kc, *wmod, *wq, *x, *c, *vc, *wo;
  unsigned char* ws;
  const u16 *wg, *wu, *wd;
  u16* out;
};


__device__ __forceinline__ rsrc_t mk_rsrc(const void* p, int bytes) { return __builtin_amdgcn_make_buffer_rsrc((void*)p, 0, bytes, 0x00020000); }
__device__ __forceinline__ float u2f(unsigned u) { return __builtin_bit_cast(float, u); }
__device__ __forceinline__ unsigned f2u(float f) { return __builtin_bit_cast(unsigned, f); }
__device__ __forceinline__ float bf_lo(unsigned u) { return u2f(u << 16); }
__device__ __forceinline__ float bf_hi(unsigned u) { return u2f(u & 0xffff0000u); }
__device__ __forceinline__ unsigned pk_bf16(float lo, float hi) {
  bf16x2_t r = {(__bf16)lo, (__bf16)hi};
  return __builtin_bit_cast(unsigned, r);
}
__device__ __forceinline__ float rbf(float f) { return (float)(__bf16)f; }
__device__ __forceinline__ unsigned nosent(unsigned u) { return u < 0xFFFFFFFEu ? u : 0xFFFFFFFEu; }
__device__ __forceinline__ float fmx(float a, float b) { return fmaxf(a, b); }
template <int CTRL> __device__ __forceinline__ float dpp(float x) {
  return __builtin_bit_cast(float, __builtin_amdgcn_mov_dpp(__builtin_bit_cast(int, x), CTRL, 0xf, 0xf, true));
}
template <int CTRL> __device__ __forceinline__ unsigned dppu(unsigned x) { return (unsigned)__builtin_amdgcn_mov_dpp((int)x, CTRL, 0xf, 0xf, true); }
constexpr int XOR1 = 0xB1, XOR2 = 0x4E, XOR7 = 0x141, ROR8 = 0x128;
__device__ __forceinline__ float x16_32_max(float x) {
  auto s = __builtin_amdgcn_permlane16_swap(f2u(x), f2u(x), false, false);
  x = fmx(u2f(s[0]), u2f(s[1]));
  auto t = __builtin_amdgcn_permlane32_swap(f2u(x), f2u(x), false, false);
  return fmx(u2f(t[0]), u2f(t[1]));
}
__device__ __forceinline__ float swap32_add(float a, float b) {
  auto t = __builtin_amdgcn_permlane32_swap(f2u(a), f2u(b), false, false);
  return u2f(t[0]) + u2f(t[1]);
}
__device__ __forceinline__ float swap16_add(float a, float b) {
  auto t = __builtin_amdgcn_permlane16_swap(f2u(a), f2u(b), false, false);
  return u2f(t[0]) + u2f(t[1]);
}
__device__ __forceinline__ float x16_32_sum(float x) { x = swap16_add(x, x); return swap32_add(x, x); }
__device__ __forceinline__ float row16_sum(float x) {
  x += dpp<XOR1>(x); x += dpp<XOR2>(x); x += dpp<XOR7>(x); x += dpp<ROR8>(x);
  return x;
}
__device__ __forceinline__ float row16_max(float x) {
  x = fmx(x, dpp<XOR1>(x)); x = fmx(x, dpp<XOR2>(x)); x = fmx(x, dpp<XOR7>(x)); x = fmx(x, dpp<ROR8>(x));
  return x;
}
__device__ __forceinline__ float row8_32_sum(float x) {
  x += dpp<XOR1>(x); x += dpp<XOR2>(x); x += dpp<XOR7>(x);
  return swap32_add(x, x);
}
__device__ __forceinline__ f32x4 mfma(v4u a, v4u b, f32x4 c) {
  return __builtin_amdgcn_mfma_f32_16x16x32_bf16(__builtin_bit_cast(bf16x8, a), __builtin_bit_cast(bf16x8, b), c, 0, 0, 0);
}
__device__ __forceinline__ float dot2bf(unsigned a, unsigned b, float c) {
  return __builtin_amdgcn_fdot2_f32_bf16(__builtin_bit_cast(bf16x2_t, a), __builtin_bit_cast(bf16x2_t, b), c, false);
}
__device__ __forceinline__ void lds_barrier() {
  asm volatile("s_waitcnt lgkmcnt(0)" ::: "memory");
  __builtin_amdgcn_s_barrier();
  asm volatile("" ::: "memory");
}
__device__ __forceinline__ int lds_peek(LAS unsigned char* L, int off) { return __builtin_amdgcn_readfirstlane(*(volatile LAS int*)(L + off)); }
template <int SLEEP = 1> __device__ __forceinline__ void lds_wait_ge(LAS unsigned char* L, int off, int want) {
#pragma nounroll
  for (unsigned spins = 0; spins < 2 * SPIN_CAP; ++spins) {
    if (lds_peek(L, off) >= want) break;
    if (SLEEP) __builtin_amdgcn_s_sleep(SLEEP);
  }
  asm volatile("" ::: "memory");
}
__device__ __forceinline__ void lds_add(LAS unsigned char* L, int off, int n) {
  asm volatile("s_mov_b64 exec, 1\n\tds_add_u32 %0, %1\n\ts_mov_b64 exec, -1" ::"v"((unsigned)(size_t)(L + off)), "v"(n) : "memory");
}
__device__ __forceinline__ v2u tr_read(LAS unsigned char* a) { return __builtin_bit_cast(v2u, __builtin_amdgcn_ds_read_tr16_b64_v4i16((LAS v4s*)a)); }
__device__ __forceinline__ void st_granule(rsrc_t rs, int off, unsigned p0, unsigned p1, unsigned t0, unsigned t1) {
  v4u v = {p0, p1, p0 ^ t0, p1 ^ t1};
  __builtin_amdgcn_raw_buffer_store_b128(v, rs, off, 0, AUX_SC1);
}
__device__ __forceinline__ bool gr_bad(v4u v, unsigned t0, unsigned t1) { return ((v.x ^ v.z ^ t0) | (v.y ^ v.w ^ t1)) != 0u; }
template <int N, int STRIDE> __device__ __forceinline__ void poll_granules(rsrc_t rs, int voff, v4u (&v)[N], unsigned t0, unsigned t1) {
#pragma nounroll
  for (unsigned spins = 0; spins < SPIN_CAP; ++spins) {
    bool bad = false;
#pragma unroll
    for (int i = 0; i < N; ++i) v[i] = __builtin_amdgcn_raw_buffer_load_b128(rs, voff, i * STRIDE, AUX_SC1);
#pragma unroll
    for (int i = 0; i < N; ++i) bad |= gr_bad(v[i], t0, t1);
    if (__builtin_amdgcn_ballot_w64(bad) == 0ull) break;
    __builtin_amdgcn_s_sleep(6);
  }
}
__device__ __forceinline__ bool has_sent(v4u v) { return max(max(v.x, v.y), max(v.z, v.w)) == SENT; }
__device__ __forceinline__ void ld_masked(v4u& v, rsrc_t rs, int voff, int soff, unsigned long long mask) {
  if (mask) asm volatile("s_mov_b64 exec, %4\n\tbuffer_load_dwordx4 %0, %1, %2, %3 offen sc1\n\ts_mov_b64 exec, -1" : "+v"(v) : "v"(voff), "s"(rs), "s"(soff), "s"(mask) : "memory");
}
template <int N> __device__ __forceinline__ void ld_wait(v4u (&v)[N]) {
  if constexpr (N == 1) asm volatile("s_waitcnt vmcnt(0)" : "+v"(v[0])::"memory");
  else if constexpr (N == 2) asm volatile("s_waitcnt vmcnt(0)" : "+v"(v[0]), "+v"(v[1])::"memory");
  else asm volatile("s_waitcnt vmcnt(0)" : "+v"(v[0]), "+v"(v[1]), "+v"(v[2]), "+v"(v[3])::"memory");
}
template <int N, int STRIDE, int SLEEP, bool FIRST = true> __device__ __forceinline__ void poll_plain(rsrc_t rs, int voff, v4u (&v)[N]) {
  if (FIRST) {
#pragma unroll
    for (int i = 0; i < N; ++i) { v[i] = v4u{0u, 0u, 0u, 0u}; ld_masked(v[i], rs, voff, i * STRIDE, ~0ull); }
  }
#pragma nounroll
  for (unsigned spins = 0; spins < SPIN_CAP; ++spins) {
    ld_wait(v);
    unsigned long long bad[N], any = 0;
#pragma unroll
    for (int i = 0; i < N; ++i) { bad[i] = __builtin_amdgcn_ballot_w64(has_sent(v[i])); any |= bad[i]; }
    if (any == 0ull) break;
    if (SLEEP) __builtin_amdgcn_s_sleep(SLEEP);
#pragma unroll
    for (int i = 0; i < N; ++i) ld_masked(v[i], rs, voff, i * STRIDE, bad[i]);
  }
  ld_wait(v);
}
__device__ __forceinline__ void wait_go(rsrc_t rs, int off, int lane) {
#pragma nounroll
  for (unsigned spins = 0; spins < SPIN_CAP; ++spins) {
    if (__builtin_amdgcn_readfirstlane(__builtin_amdgcn_raw_buffer_load_b32(rs, lane == 0 ? off : OOB, 0, AUX_SC1)) != (int)SENT) break;
    __builtin_amdgcn_s_sleep(2);
  }
}
__device__ __forceinline__ v2u load_epoch(rsrc_t rs, int lane) { return __builtin_amdgcn_raw_buffer_load_b64(rs, lane == 0 ? WS_EPOCH : OOB, 0, AUX_SC1); }
template <int NKB> __device__ __forceinline__ void load_slab(rsrc_t rw, int voff, int ldb, v4u* wr) {
#pragma unroll
  for (int i = 0; i < 2 * NKB; ++i) wr[i] = __builtin_amdgcn_raw_buffer_load_b128(rw, voff, 16 * i * ldb, AUX_W);
}
template <int N> __device__ __forceinline__ void dma_slab(rsrc_t rw, int voff, int ldb, LAS unsigned char* dst) {
#pragma unroll
  for (int i = 0; i < N; ++i) {
    unsigned keep;
    asm volatile("s_mov_b32 %0, m0\n\ts_mov_b32 m0, %3\n\ts_nop 4\n\tbuffer_load_dwordx4 %1, %2, %4 offen lds\n\ts_mov_b32 m0, %0"
                 : "=&s"(keep) : "v"(voff), "s"(rw), "s"((unsigned)(size_t)(dst + i * 1024)), "s"(16 * i * ldb) : "memory");
  }
}
template <int N> __device__ __forceinline__ void read_frags(LAS unsigned char* scr, int lane, v4u* wf) {
  const int ra = ((lane >> 4) * 8 + ((lane & 15) >> 2)) * 64 + (lane & 3) * 8 + (lane & 16) * 2;
#pragma unroll
  for (int i = 0; i < N; ++i) {
    const v2u lo = tr_read(scr + (i >> 1) * 2048 + (ra ^ ((i & 1) * 32))), hi = tr_read(scr + (i >> 1) * 2048 + (ra ^ ((i & 1) * 32)) + 256);
    wf[i] = v4u{lo.x, lo.y, hi.x, hi.y};
  }
}
template <int NKB> __device__ __forceinline__ void make_frags(LAS unsigned char* scr, int lane, v4u (&wf)[2 * NKB]) {
  constexpr int STEP = NKB >= 2 ? 2 : 1;
#pragma unroll
  for (int k0 = 0; k0 < NKB; k0 += STEP) {
#pragma unroll
    for (int i = 0; i < 2 * STEP; ++i) *(LAS v4u*)(scr + i * 1024 + lane * 16) = wf[2 * k0 + i];
    read_frags<2 * STEP>(scr, lane, wf + 2 * k0);
  }
}
template <int N> __device__ __forceinline__ f32x4 sum_parts4(LAS unsigned char* a, int stride) {
  f32x4 v[N];
#pragma unroll
  for (int i = 0; i < N; ++i) v[i] = *(LAS f32x4*)(a + i * stride);
  __builtin_amdgcn_sched_barrier(0);
#pragma unroll
  for (int s = 1; s < N; s *= 2)
#pragma unroll
    for (int i = 0; i + s < N; i += 2 * s) v[i] += v[i + s];
  return v[0];
}
__device__ __forceinline__ float sum_parts8(LAS unsigned char* a) {
  float v[8];
#pragma unroll
  for (int i = 0; i < 8; ++i) v[i] = *(LAS float*)(a + i * PSTR);
  __builtin_amdgcn_sched_barrier(0);
  return ((v[0] + v[1]) + (v[2] + v[3])) + ((v[4] + v[5]) + (v[6] + v[7]));
}
__device__ __forceinline__ unsigned residual1(const v4u (&pv)[4], float x0, float x1, float ga0, float ga1) {
  const f32x4 a = __builtin_bit_cast(f32x4, pv[0]) + __builtin_bit_cast(f32x4, pv[1]), b = __builtin_bit_cast(f32x4, pv[2]) + __builtin_bit_cast(f32x4, pv[3]);
  const float at0 = swap16_add(swap32_add(a[0], b[0]), swap32_add(a[2], b[2])), at1 = swap16_add(swap32_add(a[1], b[1]), swap32_add(a[3], b[3]));
  return pk_bf16(x0 + ga0 * at0, x1 + ga1 * at1);
}
#define PIN1(a) asm volatile("" : "+v"(a))
#define PIN2(a, b) asm volatile("" : "+v"(a), "+v"(b))
#define PIN4(a, b, c, d) asm volatile("" : "+v"(a), "+v"(b), "+v"(c), "+v"(d))
#define PIN8(a) asm volatile("" : "+v"(a[0]), "+v"(a[1]), "+v"(a[2]), "+v"(a[3]), "+v"(a[4]), "+v"(a[5]), "+v"(a[6]), "+v"(a[7]))

__global__ void __launch_bounds__(NTHR, 4) fused_block(Params p) {
  unsigned pfc = 0;
  if (threadIdx.x < 64) {
    unsigned long long pc;
    asm volatile("s_getpc_b64 %0" : "=s"(pc));
    pfc = __builtin_amdgcn_raw_buffer_load_b32(mk_rsrc((const void*)pc, CODE_PF), ((blockIdx.x & ~7u) + (threadIdx.x >> 3)) * 128, 0, 0);
  }
  __shared__ __attribute__((aligned(16))) unsigned char smem[L_END];
  LAS unsigned char* L = (LAS unsigned char*)smem;
  const int lane = threadIdx.x & 63, w = __builtin_amdgcn_readfirstlane(threadIdx.x >> 6);
  const int lw = w < 8 ? 15 - w : w - 8;
  const int wg = blockIdx.x, xg = wg & 7, r = wg >> 3;
  const int g = lane >> 4, c = lane & 15;
  const bool isM = r < 24 && (r & 6) == 4, isQ = (r & 6) == 6, isC = isM && r < 8;
  const int mi = (r >> 3) * 2 + (r & 1);
  unsigned pf0 = 0;
  if (w == 0) {
    const int dinit = isC ? -8 : (isM || isQ) ? -4 : -2, hinit = isQ ? 0 : 8;
    int iv = (lane >= 16 && (lane & 4)) ? 0x3F803F80 : 0;
    iv = lane == L_DONE / 4 ? dinit : iv;
    iv = lane == L_HOLD / 4 ? hinit : iv;
    if (lane < 48) *(LAS int*)(L + (lane < 16 ? lane * 4 : L_ZERO + ((lane - 16) >> 3) * 256 + (lane & 7) * 4)) = iv;
  } else if (w < 3) {
    pf0 = __builtin_amdgcn_raw_buffer_load_b32(mk_rsrc(p.wmod, 512 * 6144), (r * 16 + (lane >> 2)) * 6144 + ((w - 1) * 8 + ((xg + 3 * (w - 1)) & 7)) * 128, 0, AUX_W);
  }
  if (lw == 2) {
    unsigned ta, tb, tc;
    const size_t kv = ((size_t)((r >> 3) * 8 + xg) * SK + (SK - WIN_KEYS)) * 128;
    asm volatile("s_load_dword %0, %3, 0x0\n\ts_load_dword %1, %4, 0x0\n\ts_load_dword %2, %5, 0x0\n\ts_waitcnt lgkmcnt(0)" : "=&s"(ta), "=&s"(tb), "=&s"(tc)
                 : "s"(p.vc + kv), "s"(p.wo + xg * 262144), "s"(p.kc + kv));
  }
  const bool pre = !(isM || isQ) && lw < 2;
  const rsrc_t rs = mk_rsrc(p.ws, WS_END);
  const int eb = (lane >> 3) & 3;
  const int qi = mi;

  const bool erole = isM || isQ;
  if (!erole) {
    if (lw >= 3) {
      const int hx = (r >> 3) * 4 + (r & 7), id = (15 - lw) * 18 + hx;
      int t = id >> 4, row = 256 + (id & 15) * 16;
      bool on = id < 192;
      if (lw < 5) {
        const int bad = (3 - (int)((size_t)(lw == 4 ? p.wq : p.wmod) >> 7)) & 7;
        t = lw == 4 ? 4 + bad : ((3 * (bad - xg)) & 7) - 2;
        row = hx * 16;
        on = hx < 16 && t >= 0 && (lw == 4 || t < 4);
      }
      const bool mod = t < 4;
      const unsigned pf = __builtin_amdgcn_raw_buffer_load_b32(mk_rsrc(mod ? p.wmod : p.wq, !on ? 0 : mod ? 512 * 6144 : 512 * 8192), (row + (lane >> 2)) * (mod ? 6144 : 8192),
                                                               (mod ? (t + 2) * 8 + ((xg + 3 * (t + 2)) & 7) : xg * 8 + t - 4) * 128, AUX_W);
      lds_barrier();
      asm volatile("" ::"v"(pf));
      if (lw == 10 || lw == 5) lds_add(L, L_DONE, 1);
      const int hi = (r >> 3) * 4 + (r & 7), u = (lw - 12) * 18 + 17 - hi;
      if (lw >= 12 && u < 64) {
        lds_wait_ge(L, L_DONE, WPFAT + 2 * (lw - 12));
        lds_add(L, L_DONE, -1);
        unsigned pw[4];
#pragma unroll
        for (int i = 0; i < 4; ++i) pw[i] = __builtin_amdgcn_raw_buffer_load_b32(mk_rsrc(p.wo, 4096 * 1024), (xg * 4096 + u * 64 + (lane >> 2)) * 128, i * 2048, AUX_W);
        PIN4(pw[0], pw[1], pw[2], pw[3]);
        lds_add(L, L_DONE, 1);
      }
    }
  }
  if (erole) {
    if (w < 8) {
      const int ldb = isM ? 3072 * 2 : 4096 * 2;
      const int cb = isM ? mi * 8 + ((xg + 3 * mi) & 7) : xg * 8 + qi;
      const rsrc_t rw = mk_rsrc(isM ? p.wmod : p.wq, 512 * ldb);
      const int acol = 64 * w + 32 * (lane >> 5) + 4 * (lane & 7);
      const rsrc_t rx = mk_rsrc(isM ? p.c : p.x, 4096);
      const v2u xo = __builtin_amdgcn_raw_buffer_load_b64(rx, eb * 1024 + acol * 2, 0, 0);
      v4u xin[4];
      if (!isM) {
#pragma unroll
        for (int i = 0; i < 4; ++i) xin[i] = __builtin_amdgcn_raw_buffer_load_b128(rx, eb * 1024 + (lane >> 5) * 128 + (lane & 7) * 16, i * 256, 0);
      }
      const v2u ep = load_epoch(rs, lane);
      __builtin_amdgcn_s_setprio(3);
      v4u wf[8];
      {
        const int voff = (64 * w + (lane >> 3)) * ldb + cb * 128 + (lane & 7) * 16;
#pragma unroll
        for (int i = 0; i < 8; ++i) wf[i] = __builtin_amdgcn_raw_buffer_load_b128(rw, voff, 8 * i * ldb, AUX_W);
      }
      __builtin_amdgcn_sched_barrier(0);
      lds_barrier();
      if (!isC && w >= 4) lds_add(L, L_DONE, 1);
      const unsigned t0 = __builtin_amdgcn_readfirstlane(ep.x) + 0x9E3779B9u, t1 = __builtin_amdgcn_readfirstlane(ep.y) + 0x7F4A7C15u;
      float xv[4] = {bf_lo(xo.x), bf_hi(xo.x), bf_lo(xo.y), bf_hi(xo.y)};
      if (isM) {
#pragma unroll
        for (int j = 0; j < 4; ++j) xv[j] = xv[j] * __builtin_amdgcn_rcpf(1.f + __expf(-xv[j]));
      } else {
        float s = 0.f;
#pragma unroll
        for (int i = 0; i < 4; ++i)
#pragma unroll
          for (int j = 0; j < 4; ++j) s += bf_lo(xin[i][j]) + bf_hi(xin[i][j]);
        const float mean = row8_32_sum(s) * (1.f / 512.f);
        float q2 = 0.f;
#pragma unroll
        for (int i = 0; i < 4; ++i)
#pragma unroll
          for (int j = 0; j < 4; ++j) { const float d0 = bf_lo(xin[i][j]) - mean, d1 = bf_hi(xin[i][j]) - mean; q2 += d0 * d0 + d1 * d1; }
        q2 = row8_32_sum(q2);
        const float mb = rbf(mean), den = rbf(__builtin_sqrtf(rbf(rbf(q2 * (1.f / 512.f)) + 1e-5f)));
#pragma unroll
        for (int j = 0; j < 4; ++j) xv[j] = rbf(rbf(xv[j] - mb) / den);
      }
      PIN4(xv[0], xv[1], xv[2], xv[3]);
      LAS unsigned char* escr = L + L_SCR + w * SCR_B;
      {
        const int ra = (g * 8 + (c >> 2)) * 128 + (c & 3) * 8;
#pragma unroll
        for (int kb = 0; kb < 2; ++kb) {
#pragma unroll
          for (int i = 0; i < 4; ++i) *(LAS v4u*)(escr + i * 1024 + lane * 16) = wf[4 * kb + i];
#pragma unroll
          for (int e = 0; e < 4; ++e) {
            const v2u lo = tr_read(escr + ra + e * 32), hi = tr_read(escr + ra + e * 32 + 512);
            wf[4 * kb + e] = v4u{lo.x, lo.y, hi.x, hi.y};
          }
        }
      }
      if (isC && w >= 2) lds_add(L, L_DONE, 1);
      if (!isM) {
        PIN8(wf);
        lds_add(L, L_TLC, 1);
        lds_wait_ge(L, L_TLC, 8);
        v4u mv[2];
        poll_granules<2, 2048>(rs, WS_MOD + eb * MOD_B + acol * 4, mv, t0, t1);
        lds_add(L, L_HOLD, 1);
        if (w < 2) lds_add(L, L_DONE, -1);
#pragma unroll
        for (int j = 0; j < 2; ++j) {
          const unsigned sa = mv[0][j], ca = mv[1][j];
          const unsigned c1 = pk_bf16(1.f + bf_lo(ca), 1.f + bf_hi(ca));
          const unsigned tt = pk_bf16(xv[2 * j] * bf_lo(c1), xv[2 * j + 1] * bf_hi(c1));
          xv[2 * j] = bf_lo(tt) + bf_lo(sa);
          xv[2 * j + 1] = bf_hi(tt) + bf_hi(sa);
        }
      }
      *(LAS v2u*)(escr + lane * 8) = v2u{pk_bf16(xv[0], xv[1]), pk_bf16(xv[2], xv[3])};
      f32x4 acc[4];
      {
        const f32x4 z = {0.f, 0.f, 0.f, 0.f};
        const v4u a0 = *(LAS v4u*)(escr + (c & 3) * 64 + g * 16), a1 = *(LAS v4u*)(escr + 256 + (c & 3) * 64 + g * 16);
#pragma unroll
        for (int e = 0; e < 4; ++e) acc[e] = mfma(wf[4 + e], a1, mfma(wf[e], a0, z));
      }
      const f32x4 lo2 = (c & 4) ? acc[1] : acc[0], hi2 = (c & 4) ? acc[3] : acc[2];
      *(LAS f32x4*)(L + L_EPART + w * PSTR + g * 272 + c * 16) = (c & 8) ? hi2 : lo2;
      lds_add(L, L_ECNT, 1);
      if (w == 0) {
        lds_wait_ge<0>(L, L_ECNT, (isC || isQ) ? 11 : 8);
        f32x4 s[8];
#pragma unroll
        for (int i = 0; i < 8; ++i) s[i] = *(LAS f32x4*)(L + L_EPART + i * PSTR + (c & 3) * 272 + ((c >> 2) * 4 + g) * 16);
        __builtin_amdgcn_sched_barrier(0);
        const f32x4 t = ((s[0] + s[1]) + (s[2] + s[3])) + ((s[4] + s[5]) + (s[6] + s[7]));
        st_granule(rs, (isM ? WS_MOD + g * MOD_B : WS_Q + g * Q_B) + cb * 256 + c * 16, pk_bf16(t[0], t[1]), pk_bf16(t[2], t[3]), t0, t1);
        if (isC || isQ) lds_add(L, L_DONE, 2);
      }
    } else if ((isC || isQ) && w >= 13) {
#pragma unroll
      for (int k = 0; k < 2; ++k) {
        const int i = (w - 13) * 2 + k;
        const int dst = isC ? WS_P + ((mi * 8 + xg) * 6 + i) * 1024 + lane * 16 : WS_O + (xg * 8 + qi) * 4 * O_REC + i * 1024 + lane * 16 + (i * 64 + lane < 288 ? 0 : OOB);
        __builtin_amdgcn_raw_buffer_store_b128(v4u{SENT, SENT, SENT, SENT}, rs, dst, 0, AUX_SC1);
      }
      lds_barrier();
      asm volatile("s_waitcnt vmcnt(0)" ::: "memory");
      lds_add(L, L_ECNT, 1);
    }
  }
  const bool lbar = erole ? (w >= 8 && !((isC || isQ) && w >= 13)) : lw == 2;
  asm volatile("" ::"v"(pf0));

  const int ab_ = r >> 3, as_ = r & 7;
  asm volatile("" ::"v"(pfc));
  const int njob = isQ ? 13 : isM ? 15 : r < 24 ? 18 : 17;
  const int hx = ab_ * 4 + as_;
  const bool isW = erole || hx >= 16;
  const int ti = isQ ? mi : isM ? 8 + mi : hx - 2;
  unsigned t0 = 0, t1 = 0;
#pragma nounroll
  for (int job = lw; job < njob; job += 16) {
    int lane_ = lane;
    asm volatile("" : "+v"(lane_));
    const int g = lane_ >> 4, c = lane_ & 15;
    const bool last = job == njob - 1;
    v4u kr[8], vr[8];
    v2u ep;
    unsigned tref = 0;
    {
      const int tile = (job < 13 ? 8 * job : (job < 15 || r >= 24) ? 26 + 6 * job : 56 + 4 * job) + as_;
      const size_t base = ((size_t)(ab_ * 8 + xg) * SK + (SK - WIN_KEYS) + tile * 32) * 128;
      const rsrc_t rk = mk_rsrc(p.kc + base, 8192), rv = mk_rsrc(p.vc + base, 8192);
      const int koff = (c >> 2) * 256 + (4 * g + (c & 3)) * 16;
      const int idxk = job < KLEAD ? job : 2 * job - KLEAD, idxv = job + KLEAD < njob ? 2 * job + KLEAD + 1 : njob + job;
      __builtin_amdgcn_s_setprio(2);
      if (lbar) lds_barrier();
      if (!(pre && job < 16)) {
        lds_wait_ge(L, L_DONE, idxk - TOKWIN + 1);
        if (idxk >= QHOLD) lds_wait_ge(L, L_HOLD, 8);
      }
#pragma unroll
      for (int i = 0; i < 8; ++i) kr[i] = __builtin_amdgcn_raw_buffer_load_b128(rk, koff + (i & 3) * 1024, (i >> 2) * 4096, AUX_KV);
      ep = load_epoch(rs, lane_);
      __builtin_amdgcn_sched_barrier(0);
      if (pre && job < 16) lds_barrier();
      unsigned arrive = 1u;
      if (last) {
        lds_add(L, L_HUP, 1);
        lds_wait_ge(L, L_QFLAG, 1);
        asm volatile("s_mov_b64 exec, 1\n\tbuffer_atomic_add %0, off, %1, %2 sc0\n\ts_mov_b64 exec, -1" : "+v"(arrive) : "s"(rs), "s"(WS_F + xg * 256) : "memory");
      }
      if (!last) {
        asm volatile("s_waitcnt vmcnt(0)" : "+v"(arrive)::"memory");
        lds_add(L, L_DONE, 1);
        tref = (unsigned)__builtin_amdgcn_s_memrealtime();
      }
      lds_wait_ge(L, L_DONE, idxv - TOKWIN + 1 - (job >= njob - 2 ? 1 : 0));
      if (idxv >= QHOLD) lds_wait_ge(L, L_HOLD, 8);
#pragma unroll
      for (int i = 0; i < 8; ++i) vr[i] = __builtin_amdgcn_raw_buffer_load_b128(rv, lane_ * 16 + (i & 3) * 1024, (i >> 2) * 4096, AUX_KV);
      __builtin_amdgcn_sched_barrier(0);
      if (last) {
        lds_add(L, L_PFGO, 1);
        asm volatile("s_waitcnt vmcnt(8)" : "+v"(arrive)::"memory");
        lds_add(L, L_DONE, 1);
      }
      __builtin_amdgcn_raw_buffer_store_b32(0u, rs, WS_F + xg * 256 + 128 + ((lane_ == 0 && arrive == 30u) ? 0 : OOB), 0, AUX_SC1);
    }
    t0 = __builtin_amdgcn_readfirstlane(ep.x) + 0x9E3779B9u; t1 = __builtin_amdgcn_readfirstlane(ep.y) + 0x7F4A7C15u;
    const bool q_early = lds_peek(L, L_QFLAG) != 0;
    if (!q_early) {
      asm volatile("s_waitcnt vmcnt(0)" ::: "memory");
      lds_add(L, L_DONE, 1);
      if (job == 1) {
        while (lds_peek(L, L_DONE) < QPOLLAT && (unsigned)__builtin_amdgcn_s_memrealtime() - tref < (erole ? QTIME - QTROLE : QTIME)) __builtin_amdgcn_s_sleep(2);
        v4u qv[2];
        poll_granules<2, 1024>(rs, WS_Q + ab_ * Q_B + xg * 2048 + lane_ * 16, qv, t0, t1);
        *(LAS v2u*)(L + L_Q + lane_ * 8) = v2u{qv[0].x, qv[0].y};
        *(LAS v2u*)(L + L_Q + 512 + lane_ * 8) = v2u{qv[1].x, qv[1].y};
        lds_add(L, L_QFLAG, 1);
        lds_add(L, L_DONE, BUMP);
      }
      if (job >= 8) __builtin_amdgcn_s_setprio(0);
      else if (job >= 4) __builtin_amdgcn_s_setprio(1);
      lds_wait_ge(L, L_QFLAG, 1);
    }
    float om, fin_m = 0.f, fin_inv = 0.f;
    f32x4 oacc[8];
    {
      const v4u qf = *(LAS v4u*)(L + L_Q + (c >> 2) * 256 + (4 * g + (c & 3)) * 16);
      const f32x4 z = {0.f, 0.f, 0.f, 0.f};
      f32x4 d[8];
#pragma unroll
      for (int i = 0; i < 8; ++i) d[i] = mfma(kr[i], qf, z);
      float s[8];
#pragma unroll
      for (int i = 0; i < 8; ++i) s[i] = dpp<0xFF>(d[i][3]) + (dpp<0xAA>(d[i][2]) + (dpp<0x55>(d[i][1]) + dpp<0x00>(d[i][0])));
      PIN8(s);
      const bool u1 = (c & 1) != 0, u2 = (c & 2) != 0;
      float s0 = u2 ? (u1 ? s[6] : s[4]) : (u1 ? s[2] : s[0]);
      float s1 = u2 ? (u1 ? s[7] : s[5]) : (u1 ? s[3] : s[1]);
      unsigned sp = pk_bf16(s0, s1);
      sp = pk_bf16(bf_lo(sp) * 0.08838834764831845f, bf_hi(sp) * 0.08838834764831845f);
      s0 = bf_lo(sp) * 1.4426950408889634f; s1 = bf_hi(sp) * 1.4426950408889634f;
      float m = fmx(s0, s1);
      m = fmx(m, dpp<XOR1>(m)); m = fmx(m, dpp<XOR2>(m));
      m = x16_32_max(m);
      s0 = __builtin_amdgcn_exp2f(s0 - m); s1 = __builtin_amdgcn_exp2f(s1 - m);
      float l = s0 + s1;
      const unsigned pp = pk_bf16(s0, s1);
      const v4u pf = {dppu<0x00>(pp), dppu<0x55>(pp), dppu<0xAA>(pp), dppu<0xFF>(pp)};
      l += dpp<XOR1>(l); l += dpp<XOR2>(l);
      l = x16_32_sum(l);
      om = u2f(__builtin_amdgcn_ds_bpermute(g * 16, f2u(m)));
      float lse_out = 0.f;
      if (!last) {
        LAS f32x2* mlp = (LAS f32x2*)(L + L_ML + (c >> 2) * 128 + (job & 15) * 8);
        f32x2 ml = {m, l};
        if (job >= 16) {
          const f32x2 o = *mlp;
          ml[0] = fmx(m, o[0]);
          ml[1] = l * __builtin_amdgcn_exp2f(m - ml[0]) + o[1] * __builtin_amdgcn_exp2f(o[0] - ml[0]);
        }
        if ((lane_ & 51) == 0) *mlp = ml;
        lds_add(L, L_MLC, 1);
      } else {
        const float ol = u2f(__builtin_amdgcn_ds_bpermute(g * 16, f2u(l)));
        lds_wait_ge<0>(L, L_MLC, njob - 1);
        f32x2 ml = *(LAS f32x2*)(L + L_ML + lane_ * 8);
        if (c >= njob - 1) ml = f32x2{om, 0.f};
        fin_m = fmx(row16_max(ml[0]), om);
        const float lt = row16_sum(ml[1] * __builtin_amdgcn_exp2f(ml[0] - fin_m)) + ol * __builtin_amdgcn_exp2f(om - fin_m);
        lse_out = fin_m + __builtin_amdgcn_logf(lt);
        fin_inv = __builtin_amdgcn_rcpf(lt);
      }
      __builtin_amdgcn_raw_buffer_store_b32(nosent(f2u(lse_out)), rs, WS_O + ((xg * 4 + ab_) * 8 + as_) * O_REC + 1024 + g * 4 + ((c == 0 && last) ? 0 : OOB), 0, AUX_SC1);
      __builtin_amdgcn_sched_barrier(0);
      v4u vt[8];
#pragma unroll
      for (int t = 0; t < 4; ++t) {
#pragma unroll
        for (int j = 0; j < 8; ++j) vt[j][t] = __builtin_amdgcn_perm(vr[2 * t + 1][j >> 1], vr[2 * t][j >> 1], (j & 1) ? 0x07060302u : 0x05040100u);
        asm volatile("" : "+v"(vt[0][t]), "+v"(vt[1][t]), "+v"(vt[2][t]), "+v"(vt[3][t]), "+v"(vt[4][t]), "+v"(vt[5][t]), "+v"(vt[6][t]), "+v"(vt[7][t]));
      }
      if (q_early) lds_add(L, L_DONE, 1);
#pragma unroll
      for (int j = 0; j < 8; ++j) oacc[j] = mfma(pf, vt[j], z);
      __builtin_amdgcn_sched_barrier(0);
    }
    {
      __builtin_amdgcn_s_setprio(3);
      float ov[8] = {0.f, 0.f, 0.f, 0.f, 0.f, 0.f, 0.f, 0.f}, pmx = -3.0e38f;
      const int left = njob - 3 - job;
      int partner = left <= 0 ? job - 1 : job - 4;
#pragma nounroll
      for (int n = job < 4 ? 0 : left == 1 ? 3 : left == 2 ? 2 : 1; n > 0; --n) {
        const int ch = partner & 3, want = (partner >> 2) + 1;
        LAS unsigned char* mb = L + L_MRG + ch * MRG_B;
        if (left >= 3) lds_wait_ge(L, L_MSEQ + ch * 4, want - 1);
        f32x4 a0, a1; float pm;
#pragma nounroll
        for (unsigned spins = 0; spins < SPIN_CAP * 4; ++spins) {
          const int seq = *(volatile LAS int*)(L + L_MSEQ + ch * 4);
          pm = *(volatile LAS float*)(mb + 2048 + lane_ * 4);
          a0 = *(volatile LAS f32x4*)(mb + lane_ * 16); a1 = *(volatile LAS f32x4*)(mb + 1024 + lane_ * 16);
          if (__builtin_amdgcn_readfirstlane(seq) >= want) break;
        }
        const float mn = last ? fin_m : fmx(fmx(om, pmx), pm);
        const float fa = __builtin_amdgcn_exp2f(pmx - mn), fb = __builtin_amdgcn_exp2f(pm - mn) * (last ? fin_inv : 1.f);
        pmx = mn;
#pragma unroll
        for (int j = 0; j < 4; ++j) { ov[j] = ov[j] * fa + a0[j] * fb; ov[4 + j] = ov[4 + j] * fa + a1[j] * fb; }
        partner = n == 3 ? job - 3 : job - 1;
      }
      pmx = fmx(pmx, om);
      const float fo = __builtin_amdgcn_exp2f(om - pmx) * (last ? fin_inv : 1.f);
      om = pmx;
      PIN8(oacc);
#pragma unroll
      for (int j = 0; j < 8; ++j) ov[j] += oacc[j][0] * fo;
      if (!last) {
        LAS unsigned char* mb = L + L_MRG + (job & 3) * MRG_B;
        *(LAS float*)(mb + 2048 + lane_ * 4) = om;
        *(LAS f32x4*)(mb + lane_ * 16) = f32x4{ov[0], ov[1], ov[2], ov[3]};
        *(LAS f32x4*)(mb + 1024 + lane_ * 16) = f32x4{ov[4], ov[5], ov[6], ov[7]};
        lds_add(L, L_MSEQ + (job & 3) * 4, 1);
      } else {
        const v4u o = {nosent(pk_bf16(ov[0], ov[1])), nosent(pk_bf16(ov[2], ov[3])), nosent(pk_bf16(ov[4], ov[5])), nosent(pk_bf16(ov[6], ov[7]))};
        __builtin_amdgcn_raw_buffer_store_b128(o, rs, WS_O + ((xg * 4 + ab_) * 8 + as_) * O_REC + lane_ * 16, 0, AUX_SC1);
        lds_add(L, L_PFGO, 1);
      }
    }
  }
  __builtin_amdgcn_s_setprio(0);

  unsigned pf[4] = {0u, 0u, 0u, 0u};
  if (!isW && lw >= 10 && lw < 14) {
    const int u = lw - 10, ql = lane >> 2;
    lds_wait_ge(L, L_PFGO, 1);
    lds_wait_ge(L, L_DONE, 2 * njob + u - TOKWIN + 1 + BUMP);
#pragma unroll
    for (int i = 0; i < 4; ++i)
      pf[i] = __builtin_amdgcn_raw_buffer_load_b32(mk_rsrc((u & 1) ? p.wu : p.wg, FF * 1024), (hx * 32 + (u >> 1) * 16 + (ql >> 2)) * (FF * 2) + (((xg * 7) >> 1) + (ql & 3)) * 128,
                                                   i * 4 * FF * 2, AUX_W);
    if (u < 2) {
      PIN4(pf[0], pf[1], pf[2], pf[3]);
#pragma unroll
      for (int i = 0; i < 4; ++i)
        pf[i] = __builtin_amdgcn_raw_buffer_load_b32(mk_rsrc(p.wd, FF * 1024), (hx * 112 + u * 56 + (ql < 14 ? ql : 13)) * 1024 + xg * 128, i * 14 * 1024, AUX_W);
    }
  }
  LAS unsigned char* scr = L + L_SCR + w * SCR_B;
  const int slab_lane_row = lane >> 2, slab_lane_off = ((lane & 3) ^ ((lane >> 4) & 2)) * 16;
  const int ecp = 2 * (lane & 7) + (lane >> 5);
  const f32x4 z4 = {0.f, 0.f, 0.f, 0.f};
  if (isW) {
    const int v = lw - 2;
    if (v >= 0 && v < 8) {
      const int b = v >> 1, h = 2 * (v & 1) + (g & 1);
      __builtin_amdgcn_s_setprio(1);
      lds_wait_ge(L, L_DONE, 2 * njob - WSL + v);
      dma_slab<4>(mk_rsrc(p.wo, 4096 * 1024), (xg * 512 + 64 * v + slab_lane_row) * 1024 + ti * 64 + slab_lane_off, 1024, scr);
      asm volatile("s_waitcnt vmcnt(0)" ::: "memory");
      v4u wf[4], op[4];
      read_frags<4>(scr, lane, wf);
      const int rec = WS_O + (xg * 4 + b) * 8 * O_REC;
      int oo = rec + (lane >> 5) * O_REC + h * 256 + c * 16;
      unsigned lsw;
      {
        v4u lv[1] = {v4u{0u, 0u, 0u, 0u}};
        if (v == 0) {
          lds_wait_ge(L, L_HUP, 1);
          wait_go(rs, WS_F + xg * 256 + 128, lane);
          ld_masked(lv[0], rs, WS_O + (xg * 32 + lane) * O_REC + 1024, 0, 0xFFFFFFFFull);
          lds_add(L, L_TGO, 1);
        }
        lds_wait_ge(L, L_TGO, 1);
        lds_wait_ge(L, L_PFGO, 1);
#pragma unroll
        for (int i = 0; i < 4; ++i) { op[i] = v4u{0u, 0u, 0u, 0u}; ld_masked(op[i], rs, oo, i * 2 * O_REC, ~0ull); }
        if (v == 0) {
          asm volatile("s_waitcnt vmcnt(4)" : "+v"(lv[0])::"memory");
          if (lane < 32) *(LAS v4u*)(L + L_LT + lane * 16) = lv[0];
          lds_add(L, L_LSE, 1);
        }
        lds_wait_ge(L, L_LSE, 1);
        const int pr = 2 * (c & 3) + (lane >> 5);
        lsw = *(volatile LAS unsigned*)(L + L_LT + b * 128 + pr * 16 + h * 4);
        const int lo = rec + pr * O_REC + 1024 + h * 4;
        unsigned long long badl = __builtin_amdgcn_ballot_w64(lsw == SENT) & 0x000F000F000F000Full;
#pragma nounroll
        for (unsigned spins = 0; spins < SPIN_CAP && badl != 0ull; ++spins) {
          asm volatile("s_mov_b64 exec, %3\n\tbuffer_load_dword %0, %1, %2, 0 offen sc1\n\ts_mov_b64 exec, -1" : "+v"(lsw) : "v"(lo), "s"(rs), "s"(badl) : "memory");
          asm volatile("s_waitcnt vmcnt(0)" : "+v"(lsw), "+v"(op[0]), "+v"(op[1]), "+v"(op[2]), "+v"(op[3])::"memory");
          badl = __builtin_amdgcn_ballot_w64(lsw == SENT) & 0x000F000F000F000Full;
          if (badl != 0ull) __builtin_amdgcn_s_sleep(2);
#pragma unroll
          for (int i = 0; i < 4; ++i) {
            const unsigned long long rdy = (((badl >> i) & 0x10001ull) ? 0ull : 0xFFFFFFFFull) | (((badl >> (32 + i)) & 0x10001ull) ? 0ull : 0xFFFFFFFF00000000ull);
            ld_masked(op[i], rs, oo, i * 2 * O_REC, __builtin_amdgcn_ballot_w64(has_sent(op[i])) & rdy);
          }
        }
      }
      const float lse = u2f(lsw);
      float mx = fmx(lse, dpp<XOR1>(lse));
      mx = fmx(mx, dpp<XOR2>(mx));
      { auto t = __builtin_amdgcn_permlane32_swap(f2u(mx), f2u(mx), false, false); mx = fmx(u2f(t[0]), u2f(t[1])); }
      const float e = __builtin_amdgcn_exp2f(lse - mx);
      float su = e + dpp<XOR1>(e);
      su += dpp<XOR2>(su);
      const float wt = e * __builtin_amdgcn_rcpf(swap32_add(su, su));
      unsigned wp0 = pk_bf16(dpp<0x150>(wt), dpp<0x151>(wt)), wp1 = pk_bf16(dpp<0x152>(wt), dpp<0x153>(wt));
      int ia = L_WIMG + (4 * h + (c >> 2)) * 256 + b * 64 + (c & 3) * 16 + (lane >> 5) * 8, ib = L_WIMG + v * 512 + (c & 3) * 64 + g * 16;
      int ic = L_PART + v * 512 + g * 128 + c * 16;
      int id_ = L_PART + (lane >> 5) * 2048 + (lane & 3) * 128 + (((lane >> 2) & 1) * 4 + eb) * 16, pso = WS_P + ti * 4096 + (eb >> 1) * 2048 + xg * 256 + (eb & 1) * 128 + ecp * 8;
      PIN4(wp0, wp1, oo, ia); PIN4(ib, ic, id_, pso); PIN4(wf[0], wf[1], wf[2], wf[3]);
      poll_plain<4, 2 * O_REC, 1, false>(rs, oo, op);
      float a[8];
#pragma unroll
      for (int j = 0; j < 8; ++j) {
        const unsigned sel = (j & 1) ? 0x07060302u : 0x05040100u;
        a[j] = dot2bf(__builtin_amdgcn_perm(op[3][j >> 1], op[2][j >> 1], sel), wp1, dot2bf(__builtin_amdgcn_perm(op[1][j >> 1], op[0][j >> 1], sel), wp0, 0.f));
      }
#pragma unroll
      for (int j = 0; j < 4; ++j) a[j] = swap32_add(a[j], a[j + 4]);
      *(LAS v2u*)(L + ia) = v2u{pk_bf16(a[0], a[1]), pk_bf16(a[2], a[3])};
      lds_barrier();
      const v4u av0 = *(LAS v4u*)(L + ib), av1 = *(LAS v4u*)(L + ib + 256);
      const f32x4 r0 = mfma(wf[2], av1, mfma(wf[0], av0, z4)), r1 = mfma(wf[3], av1, mfma(wf[1], av0, z4));
      if (c < 8) *(LAS f32x4*)(L + ic) = (c & 4) ? r1 : r0;
      lds_barrier();
      if (v == 7) {
        const f32x4 s = sum_parts4<4>(L + id_, 512);
        __builtin_amdgcn_raw_buffer_store_b64(v2u{nosent(f2u(swap32_add(s[0], s[2]))), nosent(f2u(swap32_add(s[1], s[3])))}, rs, pso, 0, AUX_SC1);
      }
    } else {
      lds_barrier();
      lds_barrier();
    }
  } else if (hx < 14) {
    const int j = xg * 7 + (hx >> 1), bp = hx & 1, v = lw - 2;
    if (v >= 0 && v < 8) {
      __builtin_amdgcn_s_setprio(1);
      const int so = L_SCR + v * 2 * SCR_B;
      const int el = (lane >> 5) * 256 + ((lane >> 3) & 1) * 64 + (2 * (lane & 7) + (g & 1)) * 4;
      int eo = so + 8192 + el;
      const int ebat = 2 * bp + ((lane >> 3) & 1), ecol = 64 * v + 32 * (lane >> 5) + 4 * (lane & 7);
      const unsigned xw = __builtin_amdgcn_raw_buffer_load_b32(mk_rsrc(p.x, 4096), (ebat * 512 + ecol + 2 * (g & 1)) * 2, 0, 0);
      v4u mv[3];
      poll_granules<3, 2048>(rs, WS_MOD + ebat * MOD_B + (1024 + ecol) * 4, mv, t0, t1);
      const unsigned gaw = (g & 1) ? mv[0].y : mv[0].x, sbw = (g & 1) ? mv[1].y : mv[1].x, cbw = (g & 1) ? mv[2].y : mv[2].x;
      const unsigned c1w = pk_bf16(1.f + bf_lo(cbw), 1.f + bf_hi(cbw));
      *(LAS unsigned*)(L + L_EPART + v * 1024 + el) = c1w;
      *(LAS unsigned*)(L + L_EPART + v * 1024 + 512 + el) = sbw;
      float x0 = bf_lo(xw), x1 = bf_hi(xw), ga0 = bf_lo(gaw), ga1 = bf_hi(gaw), c10 = bf_lo(c1w), c11 = bf_hi(c1w);
      int ao[4];
#pragma unroll
      for (int e = 0; e < 4; ++e) ao[e] = (c >> 2) == e ? so + 8192 + (c & 1) * 64 + g * 16 : L_ZERO;
      int xo_ = c < 2 ? so + 8192 + 512 + c * 64 + g * 16 : L_ZERO + 16;
      int po = L_PART + v * PSTR + g * 272 + c * 16;
      if (v == 0) {
        wait_go(rs, WS_F + xg * 256 + 128, lane);
        __builtin_amdgcn_s_sleep(GDELAY);
        lds_add(L, L_TGO, 1);
      }
      lds_wait_ge(L, L_TGO, 1);
      const unsigned tgo = (unsigned)__builtin_amdgcn_s_memrealtime();
      lds_wait_ge(L, L_PFGO, 2);
      {
        const int wv = (64 * v + slab_lane_row) * (FF * 2) + j * 64 + slab_lane_off;
        dma_slab<4>(mk_rsrc(p.wg, 512 * FF * 2), wv, FF * 2, L + so);
        dma_slab<4>(mk_rsrc(p.wu, 512 * FF * 2), wv, FF * 2, L + so + 4096);
        asm volatile("s_waitcnt vmcnt(0)" ::: "memory");
      }
      lds_add(L, L_LSE, 1 << v);
      v4u wf[8];
      read_frags<8>(L + so, lane, wf);
      int pvo = WS_P + v * 8192 + bp * 2048 + lane * 16, ro = L_PART + ((lane >> 2) & 3) * 272 + (g * 4 + v) * 16 + (lane & 3) * 4, sto = L_STAT + v * 32 + (lane & 1) * 16;
      int sqo = L_STAT + (c & 7) * 32 + v * 20, smo = L_STAT + (c & 7) * 32 + v * 16 + 8, cso = L_WIMG + v * 512 + lane * 8;
      int gso = WS_G + j * 512 + (2 * bp + v) * 128 + lane * 2 + ((lane & 33) == 0 ? 0 : OOB);
      PIN4(sqo, smo, cso, gso);
      PIN8(wf); PIN2(pvo, sto); PIN4(x0, x1, ga0, ga1); PIN2(c10, c11); PIN4(ao[0], ao[1], ao[2], ao[3]); PIN4(eo, xo_, po, ro);
      while ((unsigned)__builtin_amdgcn_s_memrealtime() - tgo < PDELAY) __builtin_amdgcn_s_sleep(1);
      v4u pv[4];
#pragma unroll
      for (int i = 0; i < 4; ++i) { pv[i] = v4u{0u, 0u, 0u, 0u}; ld_masked(pv[i], rs, pvo, (i >> 1) * 4096 + (i & 1) * 1024, ~0ull); }
#pragma nounroll
      for (unsigned spins = 0; spins < SPIN_CAP; ++spins) {
        ld_wait(pv);
        unsigned long long bad[4], any = 0;
#pragma unroll
        for (int i = 0; i < 4; ++i) { bad[i] = __builtin_amdgcn_ballot_w64(has_sent(pv[i])); any |= bad[i]; }
        if (any == 0ull) break;
        __builtin_amdgcn_s_sleep(1);
#pragma unroll
        for (int i = 0; i < 4; ++i) ld_masked(pv[i], rs, pvo, (i >> 1) * 4096 + (i & 1) * 1024, bad[i]);
      }
      ld_wait(pv);
      const unsigned x2w = residual1(pv, x0, x1, ga0, ga1);
      *(LAS unsigned*)(L + eo) = pk_bf16(bf_lo(x2w) * c10, bf_hi(x2w) * c11);
      *(LAS unsigned*)(L + eo + 512) = x2w;
      {
        v4u av[8];
        const v4u xf0 = *(LAS v4u*)(L + xo_), xf1 = *(LAS v4u*)(L + xo_ + 256);
#pragma unroll
        for (int e = 0; e < 4; ++e) { av[2 * e] = *(LAS v4u*)(L + ao[e]); av[2 * e + 1] = *(LAS v4u*)(L + ao[e] + 256); }
        __builtin_amdgcn_sched_barrier(0);
        const f32x4 gram = mfma(xf1, xf1, mfma(xf0, xf0, z4));
        f32x4 a0 = z4, a1 = z4;
#pragma unroll
        for (int e = 0; e < 4; ++e) {
          a0 = mfma(wf[(e >> 1) * 4 + (e & 1)], av[2 * e], a0);
          a1 = mfma(wf[(e >> 1) * 4 + 2 + (e & 1)], av[2 * e + 1], a1);
        }
        if (lane < 2) *(LAS f32x4*)(L + sto) = gram;
        *(LAS f32x4*)(L + po) = a0 + a1;
      }
      lds_barrier();
      if (v < 2) {
        const float sq = *(LAS float*)(L + sqo), sm = *(LAS float*)(L + smo);
        const f32x2 cs = *(LAS f32x2*)(L + cso);
        const float t = sum_parts8(L + ro);
        float mu = sm + dpp<XOR1>(sm), vq = sq + dpp<XOR1>(sq);
        mu += dpp<XOR2>(mu); vq += dpp<XOR2>(vq);
        mu = (mu + dpp<XOR7>(mu)) * (1.f / 512.f); vq += dpp<XOR7>(vq);
        const float rstd = __builtin_amdgcn_rsqf(fmx(vq * (1.f / 512.f) - mu * mu, 0.f) + 1e-5f);
        const float pre = rbf(rstd * (t - mu * cs[0]) + cs[1]);
        auto sw = __builtin_amdgcn_permlane32_swap(f2u(pre), f2u(pre), false, false);
        const float gt = u2f(sw[0]), up = u2f(sw[1]);
        const float zz = -2.302208198f * (gt + 0.044715f * gt * gt * gt);
        const float ge = rbf(gt * __builtin_amdgcn_rcpf(1.f + __builtin_amdgcn_exp2f(zz))) * up;
        __builtin_amdgcn_raw_buffer_store_b32(nosent(pk_bf16(ge, dpp<XOR1>(ge))), rs, gso, 0, AUX_SC1);
      }
    } else {
      const int u = (lw - 10) & 7, so = L_SCR + u * 2 * SCR_B;
#pragma nounroll
      for (unsigned spins = 0; spins < 2 * SPIN_CAP; ++spins) {
        if ((lds_peek(L, L_LSE) >> u) & 1) break;
        __builtin_amdgcn_s_sleep(1);
      }
      asm volatile("" ::: "memory");
      v4u wf[8];
      read_frags<8>(L + so, lane, wf);
      v4u av[8];
#pragma unroll
      for (int e = 0; e < 4; ++e) {
        const int ao = (c >> 2) == e ? L_EPART + u * 1024 + (c & 2) * 256 + (c & 1) * 64 + g * 16 : L_ZERO;
        av[2 * e] = *(LAS v4u*)(L + ao); av[2 * e + 1] = *(LAS v4u*)(L + ao + 256);
      }
      __builtin_amdgcn_sched_barrier(0);
      f32x4 ac = z4;
#pragma unroll
      for (int i = 0; i < 8; ++i) ac = mfma(wf[(i >> 2) * 4 + (i & 1) * 2 + ((i >> 1) & 1)], av[i], ac);
      *(LAS f32x4*)(L + L_PARTC + u * PSTR + g * 272 + c * 16) = ac;
      lds_add(L, L_CST, 1);
      if (u < 2) {
        lds_wait_ge(L, L_CST, 8);
        const int ro = ((lane >> 2) & 3) * 272 + (g * 4 + u) * 16 + (lane & 3) * 4;
        *(LAS f32x2*)(L + L_WIMG + u * 512 + lane * 8) = f32x2{sum_parts8(L + L_PARTC + ro), sum_parts8(L + L_PARTC + ro + 32)};
      }
      lds_barrier();
    }
  } else {
    const int db = xg * 2 + hx - 14;
    if (w < 14) {
      lds_wait_ge(L, L_PFGO, 2);
      lds_wait_ge(L, L_LSE, 1);
      v4u wf[8];
      load_slab<4>(mk_rsrc(p.wd, FF * 1024), (128 * w + slab_lane_row) * 1024 + db * 64 + slab_lane_off, 1024, wf);
      make_frags<4>(scr, lane, wf);
      int go = WS_G + w * 2048 + (lane >> 2) * 128 + (lane & 3) * 16, ia = L_SCR + w * SCR_B + lane * 16, ib = L_SCR + w * SCR_B + (c & 3) * 64 + g * 16;
      int ic = L_PART + w * 512 + g * 128 + c * 16;
      PIN8(wf); PIN4(go, ia, ib, ic);
      lds_wait_ge(L, L_TGO, 1);
      __builtin_amdgcn_s_sleep(GGDELAY);
      v4u gv[1];
      poll_plain<1, 0, 0>(rs, go, gv);
      *(LAS v4u*)(L + ia) = gv[0];
      v4u av[4];
#pragma unroll
      for (int kb = 0; kb < 4; ++kb) av[kb] = *(LAS v4u*)(L + ib + kb * 256);
      __builtin_amdgcn_sched_barrier(0);
      f32x4 a0 = z4, a1 = z4;
#pragma unroll
      for (int kb = 0; kb < 4; ++kb) { a0 = mfma(wf[2 * kb], av[kb], a0); a1 = mfma(wf[2 * kb + 1], av[kb], a1); }
      if (c < 8) *(LAS f32x4*)(L + ic) = (c & 4) ? a1 : a0;
      lds_barrier();
    } else if (w == 14) {
      lds_wait_ge(L, L_PFGO, 2);
      wait_go(rs, WS_F + xg * 256 + 128, lane);
      __builtin_amdgcn_s_sleep(DDELAY);
      lds_add(L, L_LSE, 1);
      lds_barrier();
    } else {
      const int ebat = 2 * (lane >> 5) + ((lane >> 3) & 1);
      int oo = (ebat * 512 + 32 * db + 4 * (lane & 7) + 2 * (g & 1)) * 2;
      const unsigned xw = __builtin_amdgcn_raw_buffer_load_b32(mk_rsrc(p.x, 4096), oo, 0, 0);
      v4u mv[2];
      poll_granules<2, 1536 * 4>(rs, WS_MOD + ebat * MOD_B + (1024 + 32 * db + 4 * (lane & 7)) * 4, mv, t0, t1);
      const unsigned gaw = (g & 1) ? mv[0].y : mv[0].x, gbw = (g & 1) ? mv[1].y : mv[1].x;
      lds_wait_ge(L, L_LSE, 1);
      __builtin_amdgcn_s_sleep(DPDELAY);
      v4u pv[4];
      poll_plain<4, 1024, 4>(rs, WS_P + db * 4096 + lane * 16, pv);
      lds_add(L, L_TGO, 1);
      const unsigned x2w = residual1(pv, bf_lo(xw), bf_hi(xw), bf_lo(gaw), bf_hi(gaw));
      float x20 = bf_lo(x2w), x21 = bf_hi(x2w), gb0 = bf_lo(gbw), gb1 = bf_hi(gbw);
      int ra = L_PART + (g & 1) * 3584 + (lane & 3) * 128 + (((lane >> 2) & 1) * 4 + ebat) * 16;
      PIN4(x20, x21, gb0, gb1); PIN2(ra, oo);
      lds_barrier();
      const f32x4 s = sum_parts4<7>(L + ra, 512);
      __builtin_amdgcn_raw_buffer_store_b32(pk_bf16(x20 + gb0 * swap16_add(s[0], s[2]), x21 + gb1 * swap16_add(s[1], s[3])), mk_rsrc(p.out, 4096), oo, 0, AUX_SC1);
      if (db == 0 && lane == 0) __builtin_amdgcn_raw_buffer_store_b64(v2u{t0, t1}, rs, WS_EPOCH, 0, AUX_SC1);
    }
  }
  asm volatile("" ::"v"(pf[0]), "v"(pf[1]), "v"(pf[2]), "v"(pf[3]));
}

extern "C" void kernel_launch(void* const* d_in, const int* in_sizes, int n_in, void* d_out, int out_size, void* d_ws, size_t ws_size,
                              hipStream_t stream) {
  Params p;
  p.x = (const u16*)d_in[0]; p.kc = (const u16*)d_in[1]; p.vc = (const u16*)d_in[2]; p.wq = (const u16*)d_in[3]; p.wo = (const u16*)d_in[4];
  p.wg = (const u16*)d_in[6]; p.wu = (const u16*)d_in[7]; p.wd = (const u16*)d_in[8]; p.c = (const u16*)d_in[9]; p.wmod = (const u16*)d_in[10];
  p.out = (u16*)d_out; p.ws = (unsigned char*)d_ws;
  hipLaunchKernelGGL(fused_block, dim3(NWG), dim3(NTHR), 0, stream, p);
}
```
